```python
import torch

def _softmax(x, axis=-1):
    return torch.softmax(x, dim=axis)
def _gelu(x):
    return torch.nn.functional.gelu(x, approximate="tanh")
def _silu(x):
    return torch.nn.functional.silu(x)
def _sigmoid(x):
    return torch.sigmoid(x)
def _layernorm(x, axis=-1, eps=1e-5):
    m = x.mean(dim=axis, keepdim=True)
    v = x.var(dim=axis, keepdim=True, unbiased=False)
    return (x - m) / torch.sqrt(v + eps)
def _scatter_add(tgt, idx, src, axis=0):
    return tgt.clone().index_add_(axis, idx, src)
def _segment_sum(src, seg, n):
    out = torch.zeros((n,) + src.shape[1:], dtype=src.dtype)
    return out.index_add_(0, seg, src)
def _pad(x, pw):
    flat = tuple(v for pair in reversed(pw) for v in pair)
    return torch.nn.functional.pad(x, flat)

kernel_name = 'standalone__rmsnorm__sampling'

def setup_inputs():

    _w = (lambda a: (lambda t: t.to(torch.bfloat16) if t.is_floating_point() else t)(torch.as_tensor(a)))
    B, S, D = 1024, 8, 640
    return {
        "x": _w((lambda *s: torch.randn(*s, dtype=torch.bfloat16) * 0.05)(B, S, D)),
        "g": _w((lambda *s: torch.randn(*s, dtype=torch.bfloat16) * 0.05)(D) + 1.0),
    }

def reference(x, g):
    rms = torch.sqrt((x * x).mean(axis=-1, keepdims=True) + 1e-6)
    return x / rms * g
```

```cpp
#include <hip/hip_runtime.h>
#include <hip/hip_bf16.h>
#include <stdint.h>

typedef uint32_t u32x4 __attribute__((ext_vector_type(4)));
typedef float f32x2 __attribute__((ext_vector_type(2)));
typedef __bf16 bf16x2 __attribute__((ext_vector_type(2)));

constexpr int D = 640;
constexpr int CH = D / 8 / 16;

__device__ __forceinline__ float lo_f(uint32_t u) { return __uint_as_float(u << 16); }
__device__ __forceinline__ float hi_f(uint32_t u) { return __uint_as_float(u & 0xffff0000u); }
__device__ __forceinline__ uint32_t pack2(float a, float b) {
  f32x2 v = {a, b};
  bf16x2 r = __builtin_convertvector(v, bf16x2);
  return __builtin_bit_cast(uint32_t, r);
}
template <int CTRL>
__device__ __forceinline__ float dpp_add(float v) {
  int t = __builtin_amdgcn_update_dpp(0, __float_as_int(v), CTRL, 0xf, 0xf, true);
  return v + __int_as_float(t);
}

template <int BLOCK>
__global__ __launch_bounds__(BLOCK) void rms_kernel(const u32x4* __restrict__ x,
                                                    const u32x4* __restrict__ g,
                                                    u32x4* __restrict__ y, int rows) {
  const int tid = blockIdx.x * BLOCK + threadIdx.x;
  const int row = tid >> 4;
  const int j = tid & 15;
  if (row >= rows) return;
  const u32x4* xr = x + (size_t)row * (D / 8) + j;
  u32x4* yr = y + (size_t)row * (D / 8) + j;
  u32x4 v[CH], gv[CH];
#pragma unroll
  for (int i = 0; i < CH; ++i) v[i] = xr[i * 16];
#pragma unroll
  for (int i = 0; i < CH; ++i) gv[i] = g[j + i * 16];
  float s0 = 0.f, s1 = 0.f;
#pragma unroll
  for (int i = 0; i < CH; ++i)
#pragma unroll
    for (int k = 0; k < 4; ++k) {
      float a = lo_f(v[i][k]), b = hi_f(v[i][k]);
      s0 = fmaf(a, a, s0);
      s1 = fmaf(b, b, s1);
    }
  float ss = s0 + s1;
  ss = dpp_add<0x128>(ss);
  ss = dpp_add<0x124>(ss);
  ss = dpp_add<0x122>(ss);
  ss = dpp_add<0x121>(ss);
  const float inv = rsqrtf(ss * (1.0f / D) + 1e-6f);
#pragma unroll
  for (int i = 0; i < CH; ++i) {
    u32x4 o;
#pragma unroll
    for (int k = 0; k < 4; ++k) {
      float a = lo_f(v[i][k]) * inv * lo_f(gv[i][k]);
      float b = hi_f(v[i][k]) * inv * hi_f(gv[i][k]);
      o[k] = pack2(a, b);
    }
    yr[i * 16] = o;
  }
}

extern "C" void kernel_launch(void* const* d_in, const int* in_sizes, int n_in,
                              void* d_out, int out_size, void* d_ws, size_t ws_size,
                              hipStream_t stream) {
  const u32x4* x = (const u32x4*)d_in[0];
  const u32x4* g = (const u32x4*)d_in[1];
  u32x4* y = (u32x4*)d_out;
  const int rows = in_sizes[0] / D;
  constexpr int BLOCK = 256;
  const int grid = (rows * 16 + BLOCK - 1) / BLOCK;
  rms_kernel<BLOCK><<<grid, BLOCK, 0, stream>>>(x, g, y, rows);
}
```

```cpp
#include <hip/hip_runtime.h>
#include <hip/hip_bf16.h>
#include <stdint.h>

constexpr int D = 640;
constexpr int ROWB = D * 2;
constexpr int BLOCK = 256;

#define SS(A, X0, X1, X2, X3)                      \
  "v_dot2c_f32_bf16 v" #A ", v" #X0 ", v" #X0 "\n" \
  "v_dot2c_f32_bf16 v" #A ", v" #X1 ", v" #X1 "\n" \
  "v_dot2c_f32_bf16 v" #A ", v" #X2 ", v" #X2 "\n" \
  "v_dot2c_f32_bf16 v" #A ", v" #X3 ", v" #X3 "\n"
#define PROD(P0, P1, P2, P3, P4, P5, P6, P7, X0, X1, X2, X3, L0, L1, L2, L3, H0, H1, H2, H3) \
  "v_dot2c_f32_bf16 v" #P0 ", v" #X0 ", v" #L0 "\n"                                          \
  "v_dot2c_f32_bf16 v" #P1 ", v" #X0 ", v" #H0 "\n"                                          \
  "v_dot2c_f32_bf16 v" #P2 ", v" #X1 ", v" #L1 "\n"                                          \
  "v_dot2c_f32_bf16 v" #P3 ", v" #X1 ", v" #H1 "\n"                                          \
  "v_dot2c_f32_bf16 v" #P4 ", v" #X2 ", v" #L2 "\n"                                          \
  "v_dot2c_f32_bf16 v" #P5 ", v" #X2 ", v" #H2 "\n"                                          \
  "v_dot2c_f32_bf16 v" #P6 ", v" #X3 ", v" #L3 "\n"                                          \
  "v_dot2c_f32_bf16 v" #P7 ", v" #X3 ", v" #H3 "\n"
#define FIN(P0, P1, P2, P3, P4, P5, P6, P7, X0, X1, X2, X3, SC)                   \
  "v_pk_mul_f32 v[" #P0 ":" #P1 "], v[" #P0 ":" #P1 "], " SC " op_sel_hi:[1,0]\n" \
  "v_pk_mul_f32 v[" #P2 ":" #P3 "], v[" #P2 ":" #P3 "], " SC " op_sel_hi:[1,0]\n" \
  "v_pk_mul_f32 v[" #P4 ":" #P5 "], v[" #P4 ":" #P5 "], " SC " op_sel_hi:[1,0]\n" \
  "v_pk_mul_f32 v[" #P6 ":" #P7 "], v[" #P6 ":" #P7 "], " SC " op_sel_hi:[1,0]\n" \
  "v_cvt_pk_bf16_f32 v" #X0 ", v" #P0 ", v" #P1 "\n"                              \
  "v_cvt_pk_bf16_f32 v" #X1 ", v" #P2 ", v" #P3 "\n"                              \
  "v_cvt_pk_bf16_f32 v" #X2 ", v" #P4 ", v" #P5 "\n"                              \
  "v_cvt_pk_bf16_f32 v" #X3 ", v" #P6 ", v" #P7 "\n"
#define LDX(dst, voff, imm) "buffer_load_dwordx4 " dst ", " voff ", s[8:11], 0 offen offset:" imm " nt\n"
#define LDG(dst, voff, imm) "buffer_load_dwordx4 " dst ", " voff ", s[16:19], 0 offen offset:" imm "\n"
#define STY(src, voff, imm) "buffer_store_dwordx4 " src ", " voff ", s[12:15], 0 offen offset:" imm " sc1 nt\n"

__global__ __launch_bounds__(BLOCK) void rms640_kernel(const void* x, uint32_t xbytes, uint32_t xcfg,
                                                       void* y, uint32_t ybytes, uint32_t ycfg,
                                                       const void* g, uint32_t gbytes, uint32_t gcfg) {
  const auto kp = __builtin_amdgcn_kernarg_segment_ptr();
  const uint32_t bid = __builtin_amdgcn_workgroup_id_x();
  const uint32_t tid = __builtin_amdgcn_workitem_id_x();
  asm volatile(
      "s_load_dwordx8 s[8:15], %0, 0x0\n"
      "s_load_dwordx4 s[16:19], %0, 0x20\n"
      "v_lshlrev_b32 v1, 4, %2\n"
      "v_lshrrev_b32 v2, 6, %2\n"
      "s_mul_i32 s3, %1, 0x5000\n"
      "v_lshl_add_u32 v2, v2, 12, v1\n"
      "v_bfe_u32 v3, %2, 4, 2\n"
      "v_add_u32 v2, s3, v2\n"
      "v_and_b32 v4, 0x3ff, v1\n"
      "v_and_b32 v5, 0xf0, v1\n"
      "v_lshl_add_u32 v3, v3, 10, v2\n"
      "s_waitcnt lgkmcnt(0)\n"
      LDX("v[8:11]", "v2", "0")
      LDX("v[12:15]", "v2", "1280")
      LDX("v[16:19]", "v2", "2560")
      LDX("v[20:23]", "v2", "3840")
      LDX("v[24:27]", "v3", "1024")
      LDG("v[28:31]", "v4", "0")
      LDG("v[32:35]", "v5", "1024")
      "s_mov_b32 s4, 0xffff\n"
      "s_mov_b32 s5, 0xffff0000\n"
      "v_mov_b32 v6, 0x358637bd\n"
      "v_mov_b64 v[36:37], 0\n v_mov_b64 v[38:39], 0\n v_mov_b32 v40, 0\n"
      "v_mov_b64 v[52:53], 0\n v_mov_b64 v[54:55], 0\n v_mov_b64 v[56:57], 0\n v_mov_b64 v[58:59], 0\n"
      "v_mov_b64 v[60:61], 0\n v_mov_b64 v[62:63], 0\n v_mov_b64 v[64:65], 0\n v_mov_b64 v[66:67], 0\n"
      "v_mov_b64 v[68:69], 0\n v_mov_b64 v[70:71], 0\n v_mov_b64 v[72:73], 0\n v_mov_b64 v[74:75], 0\n"
      "v_mov_b64 v[76:77], 0\n v_mov_b64 v[78:79], 0\n v_mov_b64 v[80:81], 0\n v_mov_b64 v[82:83], 0\n"
      "v_mov_b64 v[84:85], 0\n v_mov_b64 v[86:87], 0\n v_mov_b64 v[88:89], 0\n v_mov_b64 v[90:91], 0\n"
      "s_sleep 46\n"
      "s_waitcnt vmcnt(6)\n" SS(36, 8, 9, 10, 11)
      "s_waitcnt vmcnt(5)\n" SS(37, 12, 13, 14, 15)
      "s_waitcnt vmcnt(4)\n" SS(38, 16, 17, 18, 19)
      "s_waitcnt vmcnt(3)\n" SS(39, 20, 21, 22, 23)
      "s_waitcnt vmcnt(2)\n" SS(40, 24, 25, 26, 27)
      "v_permlane32_swap_b32 v36, v38\n"
      "v_permlane32_swap_b32 v37, v39\n"
      "s_waitcnt vmcnt(1)\n"
      "v_add_f32 v36, v36, v38\n"
      "v_add_f32 v37, v37, v39\n"
      "v_and_b32 v44, s4, v28\n"
      "v_and_b32 v45, s4, v29\n"
      "v_permlane16_swap_b32 v36, v37\n"
      "v_and_b32 v46, s4, v30\n"
      "v_add_f32 v36, v36, v37\n"
      "v_add_f32 v36, v36, v40\n"
      "v_and_b32 v47, s4, v31\n"
      "v_and_b32 v28, s5, v28\n"
      "v_add_f32_dpp v36, v36, v36 row_ror:8 row_mask:0xf bank_mask:0xf\n"
      "v_and_b32 v29, s5, v29\n"
      "v_and_b32 v30, s5, v30\n"
      "v_add_f32_dpp v36, v36, v36 row_ror:4 row_mask:0xf bank_mask:0xf\n"
      "v_and_b32 v31, s5, v31\n"
      "v_dot2c_f32_bf16 v52, v8, v44\n"
      "v_add_f32_dpp v36, v36, v36 row_ror:2 row_mask:0xf bank_mask:0xf\n"
      "v_dot2c_f32_bf16 v53, v8, v28\n"
      "v_dot2c_f32_bf16 v54, v9, v45\n"
      "v_add_f32_dpp v36, v36, v36 row_ror:1 row_mask:0xf bank_mask:0xf\n"
      "v_dot2c_f32_bf16 v55, v9, v29\n"
      "v_fmac_f32 v6, 0x3acccccd, v36\n"
      "v_dot2c_f32_bf16 v56, v10, v46\n"
      "v_rsq_f32 v6, v6\n"
      "v_dot2c_f32_bf16 v57, v10, v30\n"
      "v_dot2c_f32_bf16 v58, v11, v47\n"
      "v_readlane_b32 s20, v6, 0\n"
      "v_readlane_b32 s22, v6, 16\n"
      "v_readlane_b32 s24, v6, 32\n"
      "v_readlane_b32 s26, v6, 48\n"
      "v_dot2c_f32_bf16 v59, v11, v31\n"
      PROD(60, 61, 62, 63, 64, 65, 66, 67, 12, 13, 14, 15, 44, 45, 46, 47, 28, 29, 30, 31)
      PROD(68, 69, 70, 71, 72, 73, 74, 75, 16, 17, 18, 19, 44, 45, 46, 47, 28, 29, 30, 31)
      PROD(76, 77, 78, 79, 80, 81, 82, 83, 20, 21, 22, 23, 44, 45, 46, 47, 28, 29, 30, 31)
      "s_waitcnt vmcnt(0)\n"
      "v_and_b32 v48, s4, v32\n v_and_b32 v49, s4, v33\n v_and_b32 v50, s4, v34\n v_and_b32 v51, s4, v35\n"
      "v_and_b32 v32, s5, v32\n v_and_b32 v33, s5, v33\n v_and_b32 v34, s5, v34\n v_and_b32 v35, s5, v35\n"
      PROD(84, 85, 86, 87, 88, 89, 90, 91, 24, 25, 26, 27, 48, 49, 50, 51, 32, 33, 34, 35)
      FIN(52, 53, 54, 55, 56, 57, 58, 59, 8, 9, 10, 11, "s[20:21]")
      FIN(60, 61, 62, 63, 64, 65, 66, 67, 12, 13, 14, 15, "s[22:23]")
      FIN(68, 69, 70, 71, 72, 73, 74, 75, 16, 17, 18, 19, "s[24:25]")
      FIN(76, 77, 78, 79, 80, 81, 82, 83, 20, 21, 22, 23, "s[26:27]")
      FIN(84, 85, 86, 87, 88, 89, 90, 91, 24, 25, 26, 27, "v[6:7]")
      STY("v[8:11]", "v2", "0")
      STY("v[12:15]", "v2", "1280")
      STY("v[16:19]", "v2", "2560")
      STY("v[20:23]", "v2", "3840")
      STY("v[24:27]", "v3", "1024")
      :
      : "s"(kp), "s"(bid), "v"(tid)
      : "memory",
        "s3", "s4", "s5", "s8", "s9", "s10", "s11", "s12", "s13", "s14", "s15", "s16", "s17", "s18", "s19",
        "s20", "s21", "s22", "s23", "s24", "s25", "s26", "s27",
        "v1", "v2", "v3", "v4", "v5", "v6", "v7", "v8", "v9", "v10", "v11", "v12", "v13", "v14", "v15", "v16", "v17",
        "v18", "v19", "v20", "v21", "v22", "v23", "v24", "v25", "v26", "v27", "v28", "v29", "v30", "v31", "v32", "v33",
        "v34", "v35", "v36", "v37", "v38", "v39", "v40", "v44", "v45", "v46", "v47", "v48", "v49", "v50", "v51",
        "v52", "v53", "v54", "v55", "v56", "v57", "v58", "v59", "v60", "v61", "v62", "v63", "v64", "v65", "v66", "v67",
        "v68", "v69", "v70", "v71", "v72", "v73", "v74", "v75", "v76", "v77", "v78", "v79", "v80", "v81", "v82", "v83",
        "v84", "v85", "v86", "v87", "v88", "v89", "v90", "v91");
}

__global__ __launch_bounds__(256) void rms_any_kernel(const __hip_bfloat16* __restrict__ x,
                                                      const __hip_bfloat16* __restrict__ g,
                                                      __hip_bfloat16* __restrict__ y, int d) {
  __shared__ float part[4];
  const size_t base = (size_t)blockIdx.x * d;
  float ss = 0.f;
  for (int c = threadIdx.x; c < d; c += 256) {
    const float v = __bfloat162float(x[base + c]);
    ss = fmaf(v, v, ss);
  }
  for (int o = 32; o > 0; o >>= 1) ss += __shfl_xor(ss, o, 64);
  if ((threadIdx.x & 63) == 0) part[threadIdx.x >> 6] = ss;
  __syncthreads();
  const float inv = rsqrtf((part[0] + part[1] + part[2] + part[3]) / (float)d + 1e-6f);
  for (int c = threadIdx.x; c < d; c += 256)
    y[base + c] = __float2bfloat16(__bfloat162float(x[base + c]) * __bfloat162float(g[c]) * inv);
}

extern "C" void kernel_launch(void* const* d_in, const int* in_sizes, int n_in, void* d_out, int out_size,
                              void* d_ws, size_t ws_size, hipStream_t stream) {
  const int d = in_sizes[1];
  const int rows = in_sizes[0] / d;
  if (d == D && (size_t)rows * ROWB < 0xfff00000ull) {
    const uint32_t bytes = (uint32_t)rows * ROWB;
    rms640_kernel<<<(rows + 15) / 16, BLOCK, 0, stream>>>(d_in[0], bytes, 0x00020000u, d_out, bytes, 0x00020000u,
                                                          d_in[1], (uint32_t)ROWB, 0x00020000u);
  } else {
    rms_any_kernel<<<rows, 256, 0, stream>>>((const __hip_bfloat16*)d_in[0], (const __hip_bfloat16*)d_in[1],
                                             (__hip_bfloat16*)d_out, d);
  }
}
```
